```python
import torch

def _softmax(x, axis=-1):
    return torch.softmax(x, dim=axis)

def _gelu(x):
    return torch.nn.functional.gelu(x, approximate='tanh')

def _silu(x):
    return torch.nn.functional.silu(x)

def _sigmoid(x):
    return torch.sigmoid(x)

def _layernorm(x, axis=-1, eps=1e-05):
    m = x.mean(dim=axis, keepdim=True)
    v = x.var(dim=axis, keepdim=True, unbiased=False)
    return (x - m) / torch.sqrt(v + eps)

def _scatter_add(tgt, idx, src, axis=0):
    return tgt.clone().index_add_(axis, idx, src)

def _segment_sum(src, seg, n):
    out = torch.zeros((n,) + src.shape[1:], dtype=src.dtype)
    return out.index_add_(0, seg, src)

def _pad(x, pw):
    flat = tuple((v for pair in reversed(pw) for v in pair))
    return torch.nn.functional.pad(x, flat)
kernel_name = 'standalone__dilated_atrous__vision'

def setup_inputs():

    _w = lambda a: (lambda t: t.to(torch.bfloat16) if t.is_floating_point() else t)(torch.as_tensor(a))
    H = W = 224
    C = 96
    k = 3
    return {'x': _w((lambda *s: torch.randn(*s, dtype=torch.bfloat16) * 0.05)(1, H, W, C)), 'w': _w((lambda *s: torch.randn(*s, dtype=torch.bfloat16) * 0.05)(k, k, C, C) * (1.0 / (k * C ** 0.5)))}

def reference(x, w):
    B, H, W, C = x.shape
    k = 3
    rate = 2
    pad = (k - 1) * rate // 2
    xp = _pad(x, ((0, 0), (pad, pad), (pad, pad), (0, 0)))
    out = torch.zeros_like(x)
    for i in range(k):
        for j in range(k):
            tap = xp[:, i * rate:i * rate + H, j * rate:j * rate + W, :].clone()
            out = out + torch.einsum('bhwc,co->bhwo', tap, w[i, j])
    return out
```

```cpp
#include <hip/hip_runtime.h>
#include <hip/hip_bf16.h>
#include <stdint.h>

typedef __attribute__((ext_vector_type(8))) short s16x8;
typedef __attribute__((ext_vector_type(4))) short s16x4;
typedef __attribute__((ext_vector_type(4))) float f32x4;
typedef __attribute__((ext_vector_type(2))) float f32x2;
typedef __attribute__((ext_vector_type(2))) __bf16 bf16x2;
typedef __attribute__((ext_vector_type(8))) __bf16 bf16x8;
typedef __attribute__((address_space(3))) const char* lds_cptr;

constexpr int IMG = 224;
constexpr int XROWB = 20 * 192;
constexpr int XBYTES = 18 * XROWB;
constexpr int WSLOT = 96 * 192;
constexpr int NSLOT = 5;
constexpr int WBASE = XBYTES;
constexpr unsigned XTOTAL = 224u * 224u * 192u;
constexpr unsigned WTOTAL = 9u * 96u * 192u;

#define PH1 4

__device__ __forceinline__ unsigned pack_bf16(float a, float b) {
  f32x2 v = {a, b};
  bf16x2 r = __builtin_convertvector(v, bf16x2);
  return __builtin_bit_cast(unsigned, r);
}

#define DMA16(m0v, voff, srd, soff)                                                         \
  asm volatile("s_mov_b32 m0, %0\n\ts_nop 0\n\tbuffer_load_dwordx4 %1, %2, %3 offen lds"    \
               :: "s"(m0v), "v"(voff), "s"(srd), "s"(soff) : "memory")

#define WAITVM(n) asm volatile("s_waitcnt vmcnt(%0)" :: "i"(n) : "memory")

template <int P>
__global__ __launch_bounds__(256, 1) void dilconv_kernel(const uint16_t* __restrict__ x,
                                                         const uint16_t* __restrict__ w,
                                                         uint16_t* __restrict__ out) {
  __shared__ __attribute__((aligned(16))) char lds[XBYTES + NSLOT * WSLOT];
  const int tid = threadIdx.x;
  const int lane = tid & 63;
  const int wv = __builtin_amdgcn_readfirstlane(tid >> 6);
  const int nh = wv & 1, par = wv >> 1;

  const int bid = blockIdx.x;
  const int xcd = bid & 7, idx = bid >> 3;
  const int rblk = 2 * xcd + (idx >= 14 ? 1 : 0);
  const int strip = idx >= 14 ? idx - 14 : idx;
  const int y0 = rblk * 14, x0 = strip * 16;

  const unsigned ldsb = (unsigned)(size_t)lds;
  __amdgpu_buffer_rsrc_t srdX = __builtin_amdgcn_make_buffer_rsrc((void*)x, (short)0, (int)XTOTAL, 0x00020000);
  __amdgpu_buffer_rsrc_t srdW = __builtin_amdgcn_make_buffer_rsrc((void*)w, (short)0, (int)WTOTAL, 0x00020000);

  unsigned vxE, vxO;
  {
    const int l12 = lane / 12, c1 = lane - l12 * 12;
    const int col = 5 * wv + l12;
    const int gx = x0 - 2 + col;
    const int cE = c1 ^ (((col >> 2) & 1) << 1);
    const bool ok = (unsigned)gx < (unsigned)IMG;
    vxE = ok ? (unsigned)(gx * 192 + cE * 16) : 0x7f000000u;
    vxO = ok ? (unsigned)(gx * 192 + (cE ^ 2) * 16) : 0x7f000000u;
  }
  unsigned vw[5];
#pragma unroll
  for (int n = 0; n < 5; ++n) {
    const int L = wv * 288 + n * 64 + lane;
    const int c = L / 12, ch1 = L - c * 12;
    int ch = ch1 - 2 * ((c >> 3) & 1);
    ch = ch < 0 ? ch + 12 : ch;
    vw[n] = (unsigned)((c * 12 + ch) * 16);
  }

  auto dma_x = [&](int R) {
    const int gy = y0 - 2 + R;
    const unsigned soff = ((unsigned)gy < (unsigned)IMG) ? (unsigned)gy * (224u * 192u) : XTOTAL;
    const unsigned m0v = ldsb + R * XROWB + wv * 960;
    if (lane < 60) {
      if (R & 1) DMA16(m0v, vxO, srdX, soff); else DMA16(m0v, vxE, srdX, soff);
    }
  };
  auto dma_w = [&](int tap) {
    const unsigned m0v = ldsb + WBASE + (tap % NSLOT) * WSLOT + wv * 4608;
    const unsigned soff = (unsigned)tap * (unsigned)WSLOT;
#pragma unroll
    for (int n = 0; n < 4; ++n) DMA16(m0v + n * 1024, vw[n], srdW, soff);
    if (lane < 32) DMA16(m0v + 4096, vw[4], srdW, soff);
  };

  dma_w(0);
#pragma unroll
  for (int R = 0; R < 2 * P; ++R) dma_x(R);
  dma_w(1); dma_w(2);
  dma_x(2 * P); dma_x(2 * P + 1);
  dma_w(3); dma_w(4);
#pragma unroll
  for (int R = 2 * P + 2; R < 18; ++R) dma_x(R);

  const int g = lane >> 4, p16 = lane & 15;
  lds_cptr wb[3];
  {
    const int q = (lane >> 2) & 3, p = lane & 3;
#pragma unroll
    for (int t = 0; t < 3; ++t) {
      int o = 24 * p + 8 * t + 96 * nh + 32 * (g & 1);
      o = o >= 192 ? o - 192 : o;
      wb[t] = (lds_cptr)lds + WBASE + (8 * g + q) * 192 + o;
    }
  }
  lds_cptr xb[3];
#pragma unroll
  for (int j = 0; j < 3; ++j) {
    const int sw = (par + ((2 * j + p16) >> 2)) & 1;
    xb[j] = (lds_cptr)lds + par * XROWB + (2 * j + p16) * 192 + ((g ^ (sw << 1)) * 16);
  }

  s16x8 Wf[9][3][3];
  auto load_w = [&](int tap) {
#pragma unroll
    for (int kk = 0; kk < 3; ++kk)
#pragma unroll
      for (int t = 0; t < 3; ++t) {
        lds_cptr a = wb[t] + (tap % NSLOT) * WSLOT + kk * 6144;
        s16x4 lo = __builtin_amdgcn_ds_read_tr16_b64_v4i16((__attribute__((address_space(3))) s16x4*)a);
        s16x4 hi = __builtin_amdgcn_ds_read_tr16_b64_v4i16((__attribute__((address_space(3))) s16x4*)(a + 768));
        Wf[tap][kk][t] = __builtin_shufflevector(lo, hi, 0, 1, 2, 3, 4, 5, 6, 7);
      }
  };
  auto ldx = [&](int b, int j, int kk) -> s16x8 {
    return *(const __attribute__((address_space(3))) s16x8*)(xb[j] + b * (2 * XROWB) + kk * 64);
  };

  f32x4 acc[7][3];
#pragma unroll
  for (int a = 0; a < 7; ++a)
#pragma unroll
    for (int t = 0; t < 3; ++t) acc[a][t] = (f32x4){0.f, 0.f, 0.f, 0.f};

  auto store_row = [&](int a) {
    const int gy = y0 + 2 * a + par;
    char* dst = (char*)out + (size_t)((gy * IMG + x0 + p16) * 192 + 96 * nh + 24 * g);
    uint4 v0;
    v0.x = pack_bf16(acc[a][0][0], acc[a][0][1]);
    v0.y = pack_bf16(acc[a][0][2], acc[a][0][3]);
    v0.z = pack_bf16(acc[a][1][0], acc[a][1][1]);
    v0.w = pack_bf16(acc[a][1][2], acc[a][1][3]);
    uint2 v1;
    v1.x = pack_bf16(acc[a][2][0], acc[a][2][1]);
    v1.y = pack_bf16(acc[a][2][2], acc[a][2][3]);
    *(uint4*)dst = v0;
    *(uint2*)(dst + 16) = v1;
  };

  WAITVM(38);
  __builtin_amdgcn_s_barrier();
  load_w(0);
#pragma unroll
  for (int k = 0; k < 9; ++k) {
    if (k < 8) {
      if (k < 2) WAITVM(33 - 2 * P);
      else if (k < 4) WAITVM(31 - 2 * P);
      else if (k == 4) WAITVM(15);
      else if (k == 5) WAITVM(10);
      else if (k == 6) WAITVM(5);
      else WAITVM(0);
      asm volatile("s_waitcnt lgkmcnt(0)" ::: "memory");
      __builtin_amdgcn_s_barrier();
      asm volatile("" ::: "memory");
      if (k + 5 <= 8) dma_w(k + 5);
      load_w(k + 1);
    }
    const int i = k / 3, j = k % 3;
#pragma unroll
    for (int a = 0; a < P; ++a)
#pragma unroll
      for (int kk = 0; kk < 3; ++kk) {
        s16x8 xf = ldx(a + i, j, kk);
#pragma unroll
        for (int t = 0; t < 3; ++t)
          acc[a][t] = __builtin_amdgcn_mfma_f32_16x16x32_bf16(__builtin_bit_cast(bf16x8, Wf[k][kk][t]),
                                                              __builtin_bit_cast(bf16x8, xf), acc[a][t], 0, 0, 0);
      }
  }
#pragma unroll
  for (int a = 0; a < P; ++a) store_row(a);

#pragma unroll
  for (int b = P; b < 9; ++b) {
#pragma unroll
    for (int j = 0; j < 3; ++j)
#pragma unroll
      for (int kk = 0; kk < 3; ++kk) {
        s16x8 xf = ldx(b, j, kk);
#pragma unroll
        for (int i = 0; i < 3; ++i) {
          const int a = b - i;
          if (a >= P && a <= 6) {
#pragma unroll
            for (int t = 0; t < 3; ++t)
              acc[a][t] = __builtin_amdgcn_mfma_f32_16x16x32_bf16(__builtin_bit_cast(bf16x8, Wf[i * 3 + j][kk][t]),
                                                                  __builtin_bit_cast(bf16x8, xf), acc[a][t], 0, 0, 0);
          }
        }
      }
    if (b - 2 >= P) store_row(b - 2);
  }
}

extern "C" void kernel_launch(void* const* d_in, const int* in_sizes, int n_in,
                              void* d_out, int out_size, void* d_ws, size_t ws_size,
                              hipStream_t stream) {
  const uint16_t* x = (const uint16_t*)d_in[0];
  const uint16_t* w = (const uint16_t*)d_in[1];
  uint16_t* out = (uint16_t*)d_out;
  dilconv_kernel<PH1><<<224, 256, 0, stream>>>(x, w, out);
}
```

```cpp
#include <hip/hip_runtime.h>
#include <stdint.h>
__device__ __forceinline__ float bf2f(uint16_t h){ return __builtin_bit_cast(float, (unsigned)h << 16); }
__global__ __launch_bounds__(256) void naive_kernel(const uint16_t* __restrict__ x, const uint16_t* __restrict__ w, uint16_t* __restrict__ out) {
  const unsigned long long tk0_ = __builtin_amdgcn_s_memtime();
  const int pix = blockIdx.x * 64 + (threadIdx.x >> 2), og = (threadIdx.x & 3) * 24;
  const int y = pix / 224, xx = pix % 224;
  float acc[24];
  for (int o = 0; o < 24; ++o) acc[o] = 0.f;
  for (int i = 0; i < 3; ++i) for (int j = 0; j < 3; ++j) {
    const int yy = y - 2 + 2 * i, xc = xx - 2 + 2 * j;
    if (yy < 0 || yy >= 224 || xc < 0 || xc >= 224) continue;
    const uint16_t* xp = x + (yy * 224 + xc) * 96;
    const uint16_t* wp = w + (i * 3 + j) * 9216 + og;
    for (int c = 0; c < 96; ++c) { const float xv = bf2f(xp[c]);
#pragma unroll
      for (int o = 0; o < 24; ++o) acc[o] += xv * bf2f(wp[c * 96 + o]); }
  }
  for (int o = 0; o < 24; ++o) { __bf16 b = (__bf16)acc[o]; out[pix * 96 + og + o] = __builtin_bit_cast(uint16_t, b); }
  while (__builtin_amdgcn_s_memtime() - tk0_ < 0ull) __builtin_amdgcn_s_sleep(1);
}
extern "C" void kernel_launch(void* const* d_in, const int* in_sizes, int n_in, void* d_out, int out_size, void* d_ws, size_t ws_size, hipStream_t stream) {
  naive_kernel<<<784, 256, 0, stream>>>((const uint16_t*)d_in[0], (const uint16_t*)d_in[1], (uint16_t*)d_out);
}
```

```cpp
#include <hip/hip_runtime.h>
#include <hip/hip_bf16.h>
#include <stdint.h>

typedef __attribute__((ext_vector_type(8))) short s16x8;
typedef __attribute__((ext_vector_type(4))) short s16x4;
typedef __attribute__((ext_vector_type(4))) float f32x4;
typedef __attribute__((ext_vector_type(2))) float f32x2;
typedef __attribute__((ext_vector_type(2))) __bf16 bf16x2;
typedef __attribute__((ext_vector_type(8))) __bf16 bf16x8;
typedef __attribute__((address_space(3))) const char* lds_cptr;

constexpr int IMG = 224;
constexpr int XROWB = 20 * 192;
constexpr int XBYTES = 18 * XROWB;
constexpr int WSLOT = 96 * 192;
constexpr int NSLOT = 5;
constexpr int WBASE = XBYTES;
constexpr int SCRATCH = XBYTES + NSLOT * WSLOT;
#define SLOFF(sl) ((sl) < NSLOT ? WBASE + (sl) * WSLOT : 13 * XROWB)
constexpr unsigned XTOTAL = 224u * 224u * 192u;
constexpr unsigned WTOTAL = 9u * 96u * 192u;

__device__ __forceinline__ unsigned pack_bf16(float a, float b) {
  f32x2 v = {a, b};
  bf16x2 r = __builtin_convertvector(v, bf16x2);
  return __builtin_bit_cast(unsigned, r);
}

#define DMA16(m0v, voff, srd, soff)                                                         \
  asm volatile("s_mov_b32 m0, %0\n\ts_nop 0\n\tbuffer_load_dwordx4 %1, %2, %3 offen lds"    \
               :: "s"(m0v), "v"(voff), "s"(srd), "s"(soff) : "memory")

#define WAITVM(n) asm volatile("s_waitcnt vmcnt(%0)" :: "i"(n) : "memory")
#define LGKM(n) asm volatile("s_waitcnt lgkmcnt(%0)" :: "i"(n) : "memory")
#define BAR() do { __builtin_amdgcn_s_barrier(); asm volatile("" ::: "memory"); } while (0)
#define SB __builtin_amdgcn_sched_barrier(0)

__global__ __launch_bounds__(768, 3) void dilconv_kernel(const uint16_t* __restrict__ x,
                                                         const uint16_t* __restrict__ w,
                                                         uint16_t* __restrict__ out) {
  __shared__ __attribute__((aligned(16))) char lds[SCRATCH + 1024];
  const int tid = threadIdx.x;
  const int lane = tid & 63;
  const int wv = __builtin_amdgcn_readfirstlane(tid >> 6);
  const int par = wv >= 6 ? 1 : 0, tl = wv - 6 * par;

  const int bid = blockIdx.x;
  const int xcd = bid & 7, idx = bid >> 3;
  const int rblk = 2 * xcd + (idx >= 14 ? 1 : 0);
  const int strip = idx >= 14 ? idx - 14 : idx;
  const int y0 = rblk * 14, x0 = strip * 16;

  const unsigned ldsb = (unsigned)(size_t)lds;
  __amdgpu_buffer_rsrc_t srdX = __builtin_amdgcn_make_buffer_rsrc((void*)x, (short)0, (int)XTOTAL, 0x00020000);
  __amdgpu_buffer_rsrc_t srdW = __builtin_amdgcn_make_buffer_rsrc((void*)w, (short)0, (int)WTOTAL, 0x00020000);

  unsigned vx0, vw0;
  const int xq = wv & 3, xr = wv >> 2;
  {
    const int l12 = lane / 12, c1 = lane - l12 * 12;
    const int col = 5 * xq + l12;
    const int gx = x0 - 2 + col;
    const int cE = c1 ^ (((col >> 2) & 1) << 1);
    vx0 = ((unsigned)gx < (unsigned)IMG) ? (unsigned)(gx * 192 + cE * 16) : 0x7f000000u;
    const int chr = c1 < 2 ? c1 + 10 : c1 - 2;
    vw0 = (unsigned)((l12 * 12 + ((wv & 1) ? chr : c1)) * 16);
  }

#define DMAX(n) do {                                                                              \
    const int R_ = 3 * (n) + xr;                                                                  \
    const int gy_ = y0 - 2 + R_;                                                                  \
    const unsigned soff_ = __builtin_amdgcn_readfirstlane(((unsigned)gy_ < (unsigned)IMG) ? (unsigned)gy_ * (224u * 192u) : XTOTAL); \
    const unsigned m0v_ = ldsb + R_ * XROWB + xq * 960;                                           \
    const unsigned v_ = vx0 ^ ((R_ & 1) ? 32u : 0u);                                              \
    if (lane < 60) DMA16(m0v_, v_, srdX, soff_);                                                  \
  } while (0)
#define DMAW(tap, sl) do {                                                                        \
    const unsigned m0v_ = ldsb + SLOFF(sl) + wv * 1536;                                \
    const unsigned soff_ = (unsigned)(tap) * (unsigned)WSLOT + (unsigned)wv * 1536u;              \
    if (lane < 48) { DMA16(m0v_, vw0, srdW, soff_); DMA16(m0v_ + 768, vw0, srdW, soff_ + 768); }  \
  } while (0)

  const int g = lane >> 4, p16 = lane & 15;
  lds_cptr wb;
  {
    const int q = (lane >> 2) & 3, p = lane & 3;
    int o = 32 * tl + 8 * p + 32 * (g & 1);
    o = o >= 192 ? o - 192 : o;
    wb = (lds_cptr)lds + WBASE + (8 * g + q) * 192 + o;
  }
  lds_cptr xb[3];
#pragma unroll
  for (int j = 0; j < 3; ++j) {
    const int sw = (par + ((2 * j + p16) >> 2)) & 1;
    xb[j] = (lds_cptr)lds + par * XROWB + (2 * j + p16) * 192 + ((g ^ (sw << 1)) * 16);
  }
  char* const obase = (char*)out + (size_t)(((y0 + par) * IMG + x0 + p16) * 192 + 32 * tl + 8 * g);

  s16x8 Wf[9][3];
  f32x4 acc[7];
#pragma unroll
  for (int a = 0; a < 7; ++a) acc[a] = (f32x4){0.f, 0.f, 0.f, 0.f};

#define LOADW(tap, sl, kk) do {                                                                   \
    lds_cptr a_ = wb + (SLOFF(sl) - WBASE) + (kk) * 6144;                                                \
    s16x4 lo_ = __builtin_amdgcn_ds_read_tr16_b64_v4i16((__attribute__((address_space(3))) s16x4*)a_); \
    s16x4 hi_ = __builtin_amdgcn_ds_read_tr16_b64_v4i16((__attribute__((address_space(3))) s16x4*)(a_ + 768)); \
    Wf[tap][kk] = __builtin_shufflevector(lo_, hi_, 0, 1, 2, 3, 4, 5, 6, 7);                      \
  } while (0)
#define LOADW3(tap, sl) do { LOADW(tap,sl,0); LOADW(tap,sl,1); LOADW(tap,sl,2); } while (0)
#define LDX(b, j, kk) (*(const __attribute__((address_space(3))) s16x8*)(xb[j] + (b) * (2 * XROWB) + (kk) * 64))
#define MF(a, tap, kk, xf)                                                                        \
  acc[a] = __builtin_amdgcn_mfma_f32_16x16x32_bf16(__builtin_bit_cast(bf16x8, Wf[tap][kk]),       \
                                                   __builtin_bit_cast(bf16x8, xf), acc[a], 0, 0, 0)
#define ST(a) do {                                                                                \
    char* dst_ = obase + (size_t)(a) * (2 * IMG * 192);                                           \
    *(uint2*)dst_ = make_uint2(pack_bf16(acc[a][0], acc[a][1]), pack_bf16(acc[a][2], acc[a][3])); \
  } while (0)
  const unsigned vpf = (unsigned)(lane * 16);
#define PREFW() do { if (lane < 32) DMA16(ldsb + SCRATCH, vpf, srdW, (unsigned)((idx * 12 + wv) * 512)); } while (0)

  s16x8 xf0, xf1, xf2;
  DMAX(0);
  DMAX(1);
  PREFW();
  BAR();
  DMAW(0,0);
  DMAW(3,1);
  DMAW(6,2);
  BAR();
  DMAW(1,3);
  DMAW(4,4);
  DMAW(7,5);
  WAITVM(6);
  LGKM(0); BAR();
  DMAX(2);
  DMAX(3);
  LOADW3(0,0);
  LOADW3(3,1);
  LOADW3(6,2);
  xf0 = LDX(0,0,0);
  xf1 = LDX(0,0,1);
  xf2 = LDX(0,0,2);
  SB;
  MF(0,0,0,xf0);
  SB;
  xf0 = LDX(1,0,0);
  SB;
  MF(0,0,1,xf1);
  SB;
  xf1 = LDX(1,0,1);
  SB;
  MF(0,0,2,xf2);
  SB;
  xf2 = LDX(1,0,2);
  SB;
  MF(0,3,0,xf0);
  MF(1,0,0,xf0);
  SB;
  SB;
  MF(0,3,1,xf1);
  MF(1,0,1,xf1);
  SB;
  SB;
  MF(0,3,2,xf2);
  MF(1,0,2,xf2);
  SB;
  WAITVM(2);
  LGKM(0); BAR();
  xf0 = LDX(2,0,0);
  xf1 = LDX(2,0,1);
  xf2 = LDX(2,0,2);
  LOADW(1,3,0);
  LOADW(1,3,1);
  LOADW(1,3,2);
  SB;
  MF(0,6,0,xf0);
  MF(1,3,0,xf0);
  MF(2,0,0,xf0);
  SB;
  LOADW(4,4,0);
  LOADW(4,4,1);
  LOADW(4,4,2);
  SB;
  MF(0,6,1,xf1);
  MF(1,3,1,xf1);
  MF(2,0,1,xf1);
  SB;
  LOADW(7,5,0);
  LOADW(7,5,1);
  LOADW(7,5,2);
  SB;
  MF(0,6,2,xf2);
  MF(1,3,2,xf2);
  MF(2,0,2,xf2);
  SB;
  xf0 = LDX(0,1,0);
  xf1 = LDX(0,1,1);
  xf2 = LDX(0,1,2);
  DMAW(2,0);
  SB;
  MF(0,1,0,xf0);
  SB;
  xf0 = LDX(1,1,0);
  SB;
  MF(0,1,1,xf1);
  SB;
  xf1 = LDX(1,1,1);
  SB;
  MF(0,1,2,xf2);
  SB;
  xf2 = LDX(1,1,2);
  DMAW(5,1);
  SB;
  MF(0,4,0,xf0);
  MF(1,1,0,xf0);
  SB;
  xf0 = LDX(2,1,0);
  SB;
  MF(0,4,1,xf1);
  MF(1,1,1,xf1);
  SB;
  xf1 = LDX(2,1,1);
  SB;
  MF(0,4,2,xf2);
  MF(1,1,2,xf2);
  SB;
  xf2 = LDX(2,1,2);
  DMAW(8,2);
  SB;
  MF(0,7,0,xf0);
  MF(1,4,0,xf0);
  MF(2,1,0,xf0);
  SB;
  SB;
  MF(0,7,1,xf1);
  MF(1,4,1,xf1);
  MF(2,1,1,xf1);
  SB;
  SB;
  MF(0,7,2,xf2);
  MF(1,4,2,xf2);
  MF(2,1,2,xf2);
  SB;
  WAITVM(6);
  LGKM(0); BAR();
  xf0 = LDX(3,0,0);
  xf1 = LDX(3,0,1);
  xf2 = LDX(3,0,2);
  SB;
  MF(1,6,0,xf0);
  MF(2,3,0,xf0);
  MF(3,0,0,xf0);
  SB;
  xf0 = LDX(4,0,0);
  DMAX(4);
  SB;
  MF(1,6,1,xf1);
  MF(2,3,1,xf1);
  MF(3,0,1,xf1);
  SB;
  xf1 = LDX(4,0,1);
  SB;
  MF(1,6,2,xf2);
  MF(2,3,2,xf2);
  MF(3,0,2,xf2);
  SB;
  xf2 = LDX(4,0,2);
  SB;
  MF(2,6,0,xf0);
  MF(3,3,0,xf0);
  MF(4,0,0,xf0);
  SB;
  xf0 = LDX(5,0,0);
  DMAX(5);
  SB;
  MF(2,6,1,xf1);
  MF(3,3,1,xf1);
  MF(4,0,1,xf1);
  SB;
  xf1 = LDX(5,0,1);
  SB;
  MF(2,6,2,xf2);
  MF(3,3,2,xf2);
  MF(4,0,2,xf2);
  SB;
  xf2 = LDX(5,0,2);
  SB;
  MF(3,6,0,xf0);
  MF(4,3,0,xf0);
  MF(5,0,0,xf0);
  SB;
  SB;
  MF(3,6,1,xf1);
  MF(4,3,1,xf1);
  MF(5,0,1,xf1);
  SB;
  SB;
  MF(3,6,2,xf2);
  MF(4,3,2,xf2);
  MF(5,0,2,xf2);
  SB;
  WAITVM(2);
  LGKM(0); BAR();
  xf0 = LDX(3,1,0);
  xf1 = LDX(3,1,1);
  xf2 = LDX(3,1,2);
  LOADW(2,0,0);
  SB;
  MF(1,7,0,xf0);
  MF(2,4,0,xf0);
  MF(3,1,0,xf0);
  SB;
  xf0 = LDX(4,1,0);
  LOADW(2,0,1);
  SB;
  MF(1,7,1,xf1);
  MF(2,4,1,xf1);
  MF(3,1,1,xf1);
  SB;
  xf1 = LDX(4,1,1);
  LOADW(2,0,2);
  SB;
  MF(1,7,2,xf2);
  MF(2,4,2,xf2);
  MF(3,1,2,xf2);
  SB;
  xf2 = LDX(4,1,2);
  LOADW(5,1,0);
  SB;
  MF(2,7,0,xf0);
  MF(3,4,0,xf0);
  MF(4,1,0,xf0);
  SB;
  xf0 = LDX(5,1,0);
  LOADW(5,1,1);
  SB;
  MF(2,7,1,xf1);
  MF(3,4,1,xf1);
  MF(4,1,1,xf1);
  SB;
  xf1 = LDX(5,1,1);
  LOADW(5,1,2);
  SB;
  MF(2,7,2,xf2);
  MF(3,4,2,xf2);
  MF(4,1,2,xf2);
  SB;
  xf2 = LDX(5,1,2);
  LOADW(8,2,0);
  SB;
  MF(3,7,0,xf0);
  MF(4,4,0,xf0);
  MF(5,1,0,xf0);
  SB;
  LOADW(8,2,1);
  SB;
  MF(3,7,1,xf1);
  MF(4,4,1,xf1);
  MF(5,1,1,xf1);
  SB;
  LOADW(8,2,2);
  SB;
  MF(3,7,2,xf2);
  MF(4,4,2,xf2);
  MF(5,1,2,xf2);
  SB;
  xf0 = LDX(0,2,0);
  xf1 = LDX(0,2,1);
  xf2 = LDX(0,2,2);
  SB;
  MF(0,2,0,xf0);
  SB;
  xf0 = LDX(1,2,0);
  SB;
  MF(0,2,1,xf1);
  SB;
  xf1 = LDX(1,2,1);
  SB;
  MF(0,2,2,xf2);
  SB;
  xf2 = LDX(1,2,2);
  SB;
  MF(0,5,0,xf0);
  MF(1,2,0,xf0);
  SB;
  SB;
  MF(0,5,1,xf1);
  MF(1,2,1,xf1);
  SB;
  SB;
  MF(0,5,2,xf2);
  MF(1,2,2,xf2);
  SB;
  WAITVM(0);
  LGKM(0); BAR();
  xf0 = LDX(2,2,0);
  xf1 = LDX(2,2,1);
  xf2 = LDX(2,2,2);
  SB;
  MF(0,8,0,xf0);
  MF(1,5,0,xf0);
  MF(2,2,0,xf0);
  SB;
  xf0 = LDX(3,2,0);
  SB;
  MF(0,8,1,xf1);
  MF(1,5,1,xf1);
  MF(2,2,1,xf1);
  SB;
  xf1 = LDX(3,2,1);
  SB;
  MF(0,8,2,xf2);
  MF(1,5,2,xf2);
  MF(2,2,2,xf2);
  SB;
  ST(0);
  SB;
  xf2 = LDX(3,2,2);
  SB;
  MF(1,8,0,xf0);
  MF(2,5,0,xf0);
  MF(3,2,0,xf0);
  SB;
  xf0 = LDX(4,2,0);
  SB;
  MF(1,8,1,xf1);
  MF(2,5,1,xf1);
  MF(3,2,1,xf1);
  SB;
  xf1 = LDX(4,2,1);
  SB;
  MF(1,8,2,xf2);
  MF(2,5,2,xf2);
  MF(3,2,2,xf2);
  SB;
  ST(1);
  SB;
  xf2 = LDX(4,2,2);
  SB;
  MF(2,8,0,xf0);
  MF(3,5,0,xf0);
  MF(4,2,0,xf0);
  SB;
  xf0 = LDX(5,2,0);
  SB;
  MF(2,8,1,xf1);
  MF(3,5,1,xf1);
  MF(4,2,1,xf1);
  SB;
  xf1 = LDX(5,2,1);
  SB;
  MF(2,8,2,xf2);
  MF(3,5,2,xf2);
  MF(4,2,2,xf2);
  SB;
  ST(2);
  SB;
  xf2 = LDX(5,2,2);
  SB;
  MF(3,8,0,xf0);
  MF(4,5,0,xf0);
  MF(5,2,0,xf0);
  SB;
  SB;
  MF(3,8,1,xf1);
  MF(4,5,1,xf1);
  MF(5,2,1,xf1);
  SB;
  SB;
  MF(3,8,2,xf2);
  MF(4,5,2,xf2);
  MF(5,2,2,xf2);
  SB;
  ST(3);
  SB;
  xf0 = LDX(6,0,0);
  xf1 = LDX(6,0,1);
  xf2 = LDX(6,0,2);
  SB;
  MF(4,6,0,xf0);
  MF(5,3,0,xf0);
  MF(6,0,0,xf0);
  SB;
  xf0 = LDX(6,1,0);
  SB;
  MF(4,6,1,xf1);
  MF(5,3,1,xf1);
  MF(6,0,1,xf1);
  SB;
  xf1 = LDX(6,1,1);
  SB;
  MF(4,6,2,xf2);
  MF(5,3,2,xf2);
  MF(6,0,2,xf2);
  SB;
  xf2 = LDX(6,1,2);
  SB;
  MF(4,7,0,xf0);
  MF(5,4,0,xf0);
  MF(6,1,0,xf0);
  SB;
  xf0 = LDX(6,2,0);
  SB;
  MF(4,7,1,xf1);
  MF(5,4,1,xf1);
  MF(6,1,1,xf1);
  SB;
  xf1 = LDX(6,2,1);
  SB;
  MF(4,7,2,xf2);
  MF(5,4,2,xf2);
  MF(6,1,2,xf2);
  SB;
  xf2 = LDX(6,2,2);
  SB;
  MF(4,8,0,xf0);
  MF(5,5,0,xf0);
  MF(6,2,0,xf0);
  SB;
  xf0 = LDX(7,0,0);
  SB;
  MF(4,8,1,xf1);
  MF(5,5,1,xf1);
  MF(6,2,1,xf1);
  SB;
  xf1 = LDX(7,0,1);
  SB;
  MF(4,8,2,xf2);
  MF(5,5,2,xf2);
  MF(6,2,2,xf2);
  SB;
  ST(4);
  SB;
  xf2 = LDX(7,0,2);
  SB;
  MF(5,6,0,xf0);
  MF(6,3,0,xf0);
  SB;
  xf0 = LDX(7,1,0);
  SB;
  MF(5,6,1,xf1);
  MF(6,3,1,xf1);
  SB;
  xf1 = LDX(7,1,1);
  SB;
  MF(5,6,2,xf2);
  MF(6,3,2,xf2);
  SB;
  xf2 = LDX(7,1,2);
  SB;
  MF(5,7,0,xf0);
  MF(6,4,0,xf0);
  SB;
  xf0 = LDX(7,2,0);
  SB;
  MF(5,7,1,xf1);
  MF(6,4,1,xf1);
  SB;
  xf1 = LDX(7,2,1);
  SB;
  MF(5,7,2,xf2);
  MF(6,4,2,xf2);
  SB;
  xf2 = LDX(7,2,2);
  SB;
  MF(5,8,0,xf0);
  MF(6,5,0,xf0);
  SB;
  xf0 = LDX(8,0,0);
  SB;
  MF(5,8,1,xf1);
  MF(6,5,1,xf1);
  SB;
  xf1 = LDX(8,0,1);
  SB;
  MF(5,8,2,xf2);
  MF(6,5,2,xf2);
  SB;
  ST(5);
  SB;
  xf2 = LDX(8,0,2);
  SB;
  MF(6,6,0,xf0);
  SB;
  xf0 = LDX(8,1,0);
  SB;
  MF(6,6,1,xf1);
  SB;
  xf1 = LDX(8,1,1);
  SB;
  MF(6,6,2,xf2);
  SB;
  xf2 = LDX(8,1,2);
  SB;
  MF(6,7,0,xf0);
  SB;
  xf0 = LDX(8,2,0);
  SB;
  MF(6,7,1,xf1);
  SB;
  xf1 = LDX(8,2,1);
  SB;
  MF(6,7,2,xf2);
  SB;
  xf2 = LDX(8,2,2);
  SB;
  MF(6,8,0,xf0);
  SB;
  SB;
  MF(6,8,1,xf1);
  SB;
  SB;
  MF(6,8,2,xf2);
  SB;
  ST(6);
  SB;
}

extern "C" void kernel_launch(void* const* d_in, const int* in_sizes, int n_in,
                              void* d_out, int out_size, void* d_ws, size_t ws_size,
                              hipStream_t stream) {
  const uint16_t* x = (const uint16_t*)d_in[0];
  const uint16_t* w = (const uint16_t*)d_in[1];
  uint16_t* out = (uint16_t*)d_out;
  dilconv_kernel<<<224, 768, 0, stream>>>(x, w, out);
}
```
